```python
import torch

def _softmax(x, axis=-1):
    return torch.softmax(x, dim=axis)
def _gelu(x):
    return torch.nn.functional.gelu(x, approximate="tanh")
def _silu(x):
    return torch.nn.functional.silu(x)
def _sigmoid(x):
    return torch.sigmoid(x)
def _layernorm(x, axis=-1, eps=1e-5):
    m = x.mean(dim=axis, keepdim=True)
    v = x.var(dim=axis, keepdim=True, unbiased=False)
    return (x - m) / torch.sqrt(v + eps)
def _scatter_add(tgt, idx, src, axis=0):
    return tgt.clone().index_add_(axis, idx, src)
def _segment_sum(src, seg, n):
    out = torch.zeros((n,) + src.shape[1:], dtype=src.dtype)
    return out.index_add_(0, seg, src)
def _pad(x, pw):
    flat = tuple(v for pair in reversed(pw) for v in pair)
    return torch.nn.functional.pad(x, flat)

kernel_name = 'sequential_preLN__mha_block_cached__geglu_ffn__decode'

def setup_inputs():

    _w = (lambda a: (lambda t: t.to(torch.bfloat16) if t.is_floating_point() else t)(torch.as_tensor(a)))
    def _A_setup():
        B, Sq, D = 2, 1, 640
        H, Sk, Dh = 32, 16384, 192
        return {
            "x": _w((lambda *s: torch.randn(*s, dtype=torch.bfloat16) * 0.05)(B, Sq, D)),
            "k_cache": _w((lambda *s: torch.randn(*s, dtype=torch.bfloat16) * 3.2)(B, H, Sk, Dh)),
            "v_cache": _w((lambda *s: torch.randn(*s, dtype=torch.bfloat16) * 0.4)(B, H, Sk, Dh)),
            "wq": _w((lambda *s: torch.randn(*s, dtype=torch.bfloat16) * 3.2)(D, H * Dh) * (D ** -0.5)),
            "wo": _w((lambda *s: torch.randn(*s, dtype=torch.bfloat16) * 0.4)(H * Dh, D) * ((H * Dh) ** -0.5)),
        }
    _A_in = _A_setup()

    def _B_setup():
        B, S, D, F = 2, 1, 640, 1536
        return {
            "x": _w((lambda *s: torch.randn(*s, dtype=torch.bfloat16) * 0.05)(B, S, D)),
            "w_gate": _w((lambda *s: torch.randn(*s, dtype=torch.bfloat16) * 0.05)(D, F)),
            "w_up": _w((lambda *s: torch.randn(*s, dtype=torch.bfloat16) * 0.05)(D, F)),
            "w_down": _w((lambda *s: torch.randn(*s, dtype=torch.bfloat16) * 0.05)(F, D)),
        }
    _B_in = _B_setup()

    return {
        "A_x": _A_in["x"],
        "A_k_cache": _A_in["k_cache"],
        "A_v_cache": _A_in["v_cache"],
        "A_wq": _A_in["wq"],
        "A_wo": _A_in["wo"],
        "B_x": _B_in["x"],
        "B_w_gate": _B_in["w_gate"],
        "B_w_up": _B_in["w_up"],
        "B_w_down": _B_in["w_down"],
    }

def reference(A_x, A_k_cache, A_v_cache, A_wq, A_wo, B_x, B_w_gate, B_w_up, B_w_down):
    def _A_ref(x, k_cache, v_cache, wq, wo):
        B, Sq, D = x.shape
        H, Dh = 32, 192
        q = (lambda x, *d: x.permute(*d))((x @ wq).reshape(B, Sq, H, Dh), 0, 2, 1, 3)
        scores = torch.einsum("bhqd,bhkd->bhqk", q, k_cache) * (Dh ** -0.5)
        p = _softmax(scores, axis=-1)
        o = torch.einsum("bhqk,bhkd->bhqd", p, v_cache)
        return (lambda x, *d: x.permute(*d))(o, 0, 2, 1, 3).reshape(B, Sq, H * Dh) @ wo

    def _B_ref(x, w_gate, w_up, w_down):
        return (_gelu(x @ w_gate) * (x @ w_up)) @ w_down

    x = A_x
    h = x + _A_ref(_layernorm(x, axis=-1), A_k_cache, A_v_cache, A_wq, A_wo)
    h2 = h + _B_ref(_layernorm(h, axis=-1), B_w_gate, B_w_up, B_w_down)
    return h2
```

```cpp
#include <hip/hip_runtime.h>
#include <cstdint>

#define GAS __attribute__((address_space(1)))
#define LAS __attribute__((address_space(3)))
typedef unsigned short bf16_t;
typedef unsigned long long u64;
typedef unsigned u32x4 __attribute__((ext_vector_type(4)));
typedef unsigned u32x2 __attribute__((ext_vector_type(2)));
typedef float f32x4 __attribute__((ext_vector_type(4)));
typedef float f32x2 __attribute__((ext_vector_type(2)));
typedef __bf16 bf16x2 __attribute__((ext_vector_type(2)));
typedef __bf16 bf16x8 __attribute__((ext_vector_type(8)));
typedef short s16x4 __attribute__((ext_vector_type(4)));

constexpr int D = 640, H = 32, SK = 16384, DH = 192, HD = H * DH, F = 1536;
constexpr int NWG = 256, NW = 8, NTHR = NW * 64;
constexpr int NS = 4;
constexpr int TK = 32, TBY = TK * DH * 2, W = 24;
constexpr int SCW = 1152;
constexpr int PRE = 4;
constexpr int DELAY0 = 25;
constexpr int NREP = 2;
constexpr int SKEW = 5;
constexpr int C1 = 20, C2 = 38, C3 = 60, CQ = 15;
constexpr float LOG2E = 1.4426950408889634f;
constexpr unsigned SENT = 0xFFFFFFFFu;
constexpr unsigned SPIN_CAP = 1u << 19;
constexpr int AUX_SC1 = 16, AUX_NT = 2;

constexpr int WS_EPOCH = 0, WS_FLAGS = 4096, WS_QG = WS_FLAGS + 128 * 128, WS_PA = WS_QG + 2 * HD * 8, PA_STRIDE = 1024,
              WS_PB = WS_PA + NWG * PA_STRIDE, PB_REP = 8 * 2 * D * 4, WS_ACT = WS_PB + NREP * PB_REP, ACT_REP = 2 * F * 2, WS_BYTES = WS_ACT + NREP * ACT_REP;
constexpr int L_MISC = 0, L_QBF = 64, L_OB = L_QBF + DH * 2, L_H = L_OB + NW * 2 * 96 * 2, L_ST = L_H + 2 * D * 4, L_RED = L_ST + NW * 8, L_H3 = L_RED + 128 * NW * 4,
              L_WA = 14336, L_SC = L_WA + 98304, L_MRG = L_SC + NS * SCW * 4, MRG_STRIDE = (DH + 4) * 4, L_BYTES = L_MRG + NS * MRG_STRIDE;
static_assert(L_H3 + 2 * 32 * 4 <= L_WA - 256, "");
static_assert(L_BYTES <= 160 * 1024 && L_BYTES > 80 * 1024, "one workgroup per CU");
static_assert(L_MRG % 16 == 0 && L_QBF % 16 == 0 && L_OB % 16 == 0 && L_H % 16 == 0 && L_ST % 16 == 0 && L_RED % 16 == 0 && L_H3 % 16 == 0 && L_MISC % 16 == 0, "");

__device__ __forceinline__ float bflo(unsigned v) { return __builtin_bit_cast(float, v << 16); }
__device__ __forceinline__ float bfhi(unsigned v) { return __builtin_bit_cast(float, v & 0xffff0000u); }
__device__ __forceinline__ float rbf(float f) { return (float)(__bf16)f; }
__device__ __forceinline__ unsigned f2bf(float f) { return (unsigned)__builtin_bit_cast(unsigned short, (__bf16)f); }
__device__ __forceinline__ unsigned pk2(float lo, float hi) { bf16x2 v = {(__bf16)lo, (__bf16)hi}; return __builtin_bit_cast(unsigned, v); }
__device__ __forceinline__ unsigned fbits(float f) { return __builtin_bit_cast(unsigned, f); }
__device__ __forceinline__ float bitsf(unsigned u) { return __builtin_bit_cast(float, u); }
__device__ __forceinline__ unsigned umax(unsigned a, unsigned b) { return a > b ? a : b; }
__device__ __forceinline__ unsigned umin(unsigned a, unsigned b) { return a < b ? a : b; }
__device__ __forceinline__ unsigned desent(unsigned u) { return umin(u, SENT - 1u); }
__device__ __forceinline__ u32x4 desent4(f32x4 v) { return u32x4{desent(fbits(v[0])), desent(fbits(v[1])), desent(fbits(v[2])), desent(fbits(v[3]))}; }
__device__ __forceinline__ unsigned umax4(unsigned m, u32x4 v) { return umax(umax(umax(m, v[0]), v[1]), umax(v[2], v[3])); }
__device__ __forceinline__ float fin(float m) { return m == -INFINITY ? 0.f : m; }
__device__ __forceinline__ float dot2(unsigned k, unsigned q, float acc) {
  return __builtin_amdgcn_fdot2_f32_bf16(__builtin_bit_cast(bf16x2, k), __builtin_bit_cast(bf16x2, q), acc, false);
}
template <int CTRL> __device__ __forceinline__ float dpp(float x) {
  return __builtin_bit_cast(float, __builtin_amdgcn_mov_dpp(__builtin_bit_cast(int, x), CTRL, 0xf, 0xf, true));
}
constexpr int XOR1 = 0xB1, XOR2 = 0x4E, HMIRROR = 0x141, MIRROR = 0x140, ROR8 = 0x128;
__device__ __forceinline__ float swap16_sum(float a, float b) { auto s = __builtin_amdgcn_permlane16_swap(fbits(a), fbits(b), false, false); return bitsf(s[0]) + bitsf(s[1]); }
__device__ __forceinline__ float swap32_sum(float a, float b) { auto s = __builtin_amdgcn_permlane32_swap(fbits(a), fbits(b), false, false); return bitsf(s[0]) + bitsf(s[1]); }
__device__ __forceinline__ float row_sum(float v) { v += dpp<XOR1>(v); v += dpp<XOR2>(v); v += dpp<HMIRROR>(v); v += dpp<MIRROR>(v); return v; }
__device__ __forceinline__ float half_sum(float v) { v = row_sum(v); return swap16_sum(v, v); }
__device__ __forceinline__ float wave_sum(float v) { v = half_sum(v); return swap32_sum(v, v); }
__device__ __forceinline__ float wave_max(float v) {
  v = fmaxf(v, dpp<XOR1>(v)); v = fmaxf(v, dpp<XOR2>(v)); v = fmaxf(v, dpp<HMIRROR>(v)); v = fmaxf(v, dpp<MIRROR>(v));
  auto s = __builtin_amdgcn_permlane16_swap(fbits(v), fbits(v), false, false); v = fmaxf(bitsf(s[0]), bitsf(s[1]));
  auto t = __builtin_amdgcn_permlane32_swap(fbits(v), fbits(v), false, false); return fmaxf(bitsf(t[0]), bitsf(t[1]));
}
__device__ __forceinline__ void glds16(const void* gsrc, unsigned lds_dst) {
  unsigned keep;
  asm volatile("s_mov_b32 %0, m0\n\ts_mov_b32 m0, %2\n\ts_nop 0\n\tglobal_load_lds_dwordx4 %1, off\n\ts_mov_b32 m0, %0" : "=&s"(keep) : "v"(gsrc), "s"(lds_dst) : "memory");
}
__device__ __forceinline__ void glds4b(__amdgpu_buffer_rsrc_t rs, int voff, unsigned lds_dst) {
  unsigned keep;
  asm volatile("s_mov_b32 %0, m0\n\ts_mov_b32 m0, %3\n\ts_nop 0\n\tbuffer_load_dword %1, %2, 0 offen lds\n\ts_mov_b32 m0, %0" : "=&s"(keep) : "v"(voff), "s"(rs), "s"(lds_dst) : "memory");
}
#define DRAIN() asm volatile("s_waitcnt vmcnt(0)" ::: "memory")
#define LGKM0() asm volatile("s_waitcnt lgkmcnt(0)" ::: "memory")
#define VMWAIT(n) asm volatile("s_waitcnt vmcnt(%0)" :: "i"(n) : "memory")
#define PIN(v) asm volatile("" : "+v"(v))
#define WAVE_SYNC() do { __builtin_amdgcn_fence(__ATOMIC_ACQ_REL, "wavefront"); __builtin_amdgcn_wave_barrier(); } while (0)
__device__ __forceinline__ float fdiv(float a, float b, float rb) { const float q = a * rb; return __builtin_fmaf(__builtin_fmaf(-q, b, a), rb, q); }
__device__ __forceinline__ bf16x8 trfrag(LAS unsigned char* p, int rowb) {
  const s16x4 lo = __builtin_amdgcn_ds_read_tr16_b64_v4i16((LAS s16x4*)p), hi = __builtin_amdgcn_ds_read_tr16_b64_v4i16((LAS s16x4*)(p + 4 * rowb));
  typedef short s16x8 __attribute__((ext_vector_type(8)));
  const s16x8 v = {lo[0], lo[1], lo[2], lo[3], hi[0], hi[1], hi[2], hi[3]};
  return __builtin_bit_cast(bf16x8, v);
}
__device__ __forceinline__ f32x4 mfma(u32x4 a, bf16x8 b, f32x4 c) { return __builtin_amdgcn_mfma_f32_16x16x32_bf16(__builtin_bit_cast(bf16x8, a), b, c, 0, 0, 0); }
__device__ __forceinline__ f32x4 tree8(const u32x4 (&p)[8]) {
  auto f = [&](int n) { return __builtin_bit_cast(f32x4, p[n]); };
  return ((f(0) + f(1)) + (f(2) + f(3))) + ((f(4) + f(5)) + (f(6) + f(7)));
}
__device__ __forceinline__ float sum8(f32x4 a, f32x4 b) { const f32x4 t = a + b; return (t[0] + t[1]) + (t[2] + t[3]); }

__device__ __forceinline__ int reduce_col(int lane, int n) {
  const int idx = 3 * ((lane >> 3) & 1) + 6 * ((lane >> 4) & 1) + 12 * (lane >> 5) + n;
  return ((idx >> 3) * 8 + (lane & 7)) * 8 + (idx & 7);
}
__device__ __forceinline__ void reduce_rows(const f32x2 (&acc)[12], float scale, LAS unsigned char* L, const int (&dst)[3], int lane) {
  float u[12], y[6];
#pragma unroll
  for (int n = 0; n < 12; ++n) u[n] = swap32_sum(acc[n >> 1][n & 1], acc[(n + 12) >> 1][n & 1]);
#pragma unroll
  for (int n = 0; n < 6; ++n) y[n] = swap16_sum(u[n], u[n + 6]);
#pragma unroll
  for (int n = 0; n < 3; ++n) {
    const float t1 = y[n] + dpp<ROR8>(y[n]), t2 = y[n + 3] + dpp<ROR8>(y[n + 3]);
    *(LAS float*)(L + dst[n]) = ((lane & 8) ? t2 : t1) * scale;
  }
}
__device__ __forceinline__ void fma_row(f32x2 (&acc)[12], int j, u32x4 v, float p) {
  const f32x2 pp = {p, p};
#pragma unroll
  for (int e = 0; e < 4; ++e) { const f32x2 vf = {bflo(v[e]), bfhi(v[e])}; acc[j * 4 + e] = __builtin_elementwise_fma(pp, vf, acc[j * 4 + e]); }
}
template <int NK> __device__ __forceinline__ void ln_store(float mean, float var, const float (&hk)[NK], LAS unsigned char* ob, int lane) {
  const float m = rbf(mean), den = rbf(__builtin_amdgcn_sqrtf(rbf(rbf(var) + 1e-5f))), rden = __builtin_amdgcn_rcpf(den);
#pragma unroll
  for (int ks = 0; ks < NK; ++ks) ((LAS unsigned short*)(ob + (lane >> 5) * (NK * 64)))[ks * 32 + (lane & 31)] = (unsigned short)f2bf(fdiv(rbf(hk[ks] - m), den, rden));
  WAVE_SYNC();
}
template <int NK, int NCT> __device__ __forceinline__ void gemv2(LAS unsigned char* ob, const bf16x8 (&bw)[NK][NCT], int nks, LAS float* rd, int lane) {
  f32x4 cc[NCT];
  u32x4 af[NK];
#pragma unroll
  for (int ct = 0; ct < NCT; ++ct) cc[ct] = f32x4{0.f, 0.f, 0.f, 0.f};
#pragma unroll
  for (int ks = 0; ks < NK; ++ks) af[ks] = *(const LAS u32x4*)(ob + (lane & 1) * (NK * 64) + ks * 64 + (lane >> 4) * 16);
  __builtin_amdgcn_sched_barrier(0);
#pragma unroll
  for (int ks = 0; ks < NK; ++ks) asm volatile("" : "+v"(af[ks]));
#pragma unroll
  for (int ks = 0; ks < NK; ++ks) if (ks < nks) {
#pragma unroll
    for (int ct = 0; ct < NCT; ++ct) cc[ct] = mfma(af[ks], bw[ks][ct], cc[ct]); }
  if (lane < 16) {
#pragma unroll
    for (int ct = 0; ct < NCT; ++ct) { rd[(ct * 32 + lane) * 8] = cc[ct][0]; rd[(ct * 32 + 16 + lane) * 8] = cc[ct][1]; } }
}

template <int N, typename LD> __device__ __forceinline__ void poll(u32x4 (&v)[N], LD ld) {
  for (unsigned spin = 0; spin < SPIN_CAP; ++spin) {
    unsigned mxv = 0u;
#pragma unroll
    for (int n = 0; n < N; ++n) v[n] = ld(n);
#pragma unroll
    for (int n = 0; n < N; ++n) mxv = umax4(mxv, v[n]);
    if (!__any(mxv == SENT)) break;
    __builtin_amdgcn_s_sleep(1);
  }
}

#define OFF(u) (((u) / 3) * 3072 + ((u) % 3) * 128)

struct Plan { u64 role[4], ord[4], tiles[8]; };
constexpr Plan make_plan() {
  Plan P{};
  for (int gp = 0; gp < 2; ++gp) {
    const char* map = gp == 0 ? "111c" "ab32" "1c20" "ab23" "11c1" "ab32" "c111" "ab22" : "111c" "ab32" "1c01" "ab22" "11c1" "ab23" "c110" "ab22";
    int cnt[4] = {0, 0, 0, 0};
    for (int quad = 0; quad < 8; ++quad) {
      int cost[4] = {0, 0, 0, 0}, sum = 0, d[4] = {0, 0, 0, 0}, dsum = 0;
      for (int e = 0; e < 4; ++e) {
        const int idx = gp * 32 + quad * 4 + e, ch = map[quad * 4 + e], role = ch >= 'a' ? 0 : ch - '0', qp = ch >= 'a' ? ch - 'a' + 1 : 0;
        P.role[idx >> 4] |= (u64)(role + 4 * qp) << ((idx & 15) * 4);
        P.ord[idx >> 4] |= (u64)cnt[role] % 16 << ((idx & 15) * 4);
        ++cnt[role];
        cost[e] = role == 1 ? C1 : role == 2 ? C2 : role == 3 ? C3 : qp ? CQ : 0;
        sum += cost[e];
      }
      for (int e = 0; e < 4; ++e) { const int t = sum - 4 * cost[e]; d[e] = (t + (t >= 0 ? 20 : -20)) / 40; dsum += d[e]; }
      while (dsum != 0) {
        const int sg = dsum > 0 ? -1 : 1;
        int best = 0, berr = 1 << 30;
        for (int e = 0; e < 4; ++e) { int err = (d[e] + sg) * 40 - (sum - 4 * cost[e]); err = err < 0 ? -err : err; if (err < berr) { berr = err; best = e; } }
        d[best] += sg; dsum += sg;
      }
      for (int e = 0; e < 4; ++e) { const int idx = gp * 32 + quad * 4 + e; P.tiles[idx >> 3] |= (u64)(128 + ((e & 1) ? -SKEW : SKEW) + d[e]) << ((idx & 7) * 8); }
    }
  }
  return P;
}
constexpr Plan PLAN = make_plan();
constexpr bool plan_ok() {
  for (int q = 0; q < 16; ++q) { int sum = 0; for (int e = 0; e < 4; ++e) { const int n = (int)(PLAN.tiles[q >> 1] >> (((q & 1) * 4 + e) * 8)) & 255; if (n < 96 || ((n + 3) / 4 | 1) * TK > SCW) return false; sum += n; } if (sum * TK != SK) return false; }
  return true; }
static_assert(plan_ok(), "the four ranges cover the keys; the scores fit; a range is longer than the largest start offset (85 tiles)");
#define PICK1(v, i, n, c) do { v = (i) == (n) ? (c) : v; asm volatile("" : "+s"(v)); } while (0)
__device__ __forceinline__ u64 plan_role(int i) { u64 v = PLAN.role[0]; PICK1(v, i, 1, PLAN.role[1]); PICK1(v, i, 2, PLAN.role[2]); PICK1(v, i, 3, PLAN.role[3]); return v; }
__device__ __forceinline__ u64 plan_ord(int i) { u64 v = PLAN.ord[0]; PICK1(v, i, 1, PLAN.ord[1]); PICK1(v, i, 2, PLAN.ord[2]); PICK1(v, i, 3, PLAN.ord[3]); return v; }
__device__ __forceinline__ u64 plan_tiles(int i) { u64 v = PLAN.tiles[0]; PICK1(v, i, 1, PLAN.tiles[1]); PICK1(v, i, 2, PLAN.tiles[2]); PICK1(v, i, 3, PLAN.tiles[3]);
  PICK1(v, i, 4, PLAN.tiles[4]); PICK1(v, i, 5, PLAN.tiles[5]); PICK1(v, i, 6, PLAN.tiles[6]); PICK1(v, i, 7, PLAN.tiles[7]); return v; }

__global__ void __launch_bounds__(NTHR, 2)
fused_block(const bf16_t* __restrict__ x, const bf16_t* __restrict__ Kc, const bf16_t* __restrict__ Vc, const bf16_t* __restrict__ wq,
            const bf16_t* __restrict__ wo, const bf16_t* __restrict__ wg, const bf16_t* __restrict__ wu, const bf16_t* __restrict__ wd,
            bf16_t* __restrict__ out, unsigned char* ws) {
  extern __shared__ __attribute__((aligned(16))) unsigned char smem[];
  LAS unsigned char* L = (LAS unsigned char*)smem;
  const unsigned lbase = (unsigned)(uintptr_t)L;
  if ((threadIdx.x >> 6) == 3 && ((blockIdx.x >> 3) == 0 || (blockIdx.x >> 3) == 3)) {
    const char* pc; int clen;
    asm volatile("s_getpc_b64 %0\n.Lfb_code_begin:\n\ts_mov_b32 %1, .Lfb_code_end-.Lfb_code_begin" : "=s"(pc), "=s"(clen));
    const __amdgpu_buffer_rsrc_t rc = __builtin_amdgcn_make_buffer_rsrc((void*)pc, 0, clen, 0x00020000);
#pragma unroll
    for (int n = 0; n < 8; ++n) glds4b(rc, (n * 64 + (int)(threadIdx.x & 63)) * 64, lbase + L_WA - 256);
  }
  __builtin_amdgcn_sched_barrier(0);
  const int bid = blockIdx.x, par = bid & 1, pr = (bid >> 1) & 3, bi = bid >> 3, hp = bi >> 2, h = hp * 4 + pr, b = (bi >> 1) & 1, s = (bi & 1) * 2 + par, jq = b * 4 + s;
  const int g = pr * 2 + (hp >> 2);
  auto ghead = [&](int k) { return ((g & 1) * 4 + k) * 4 + (g >> 1); };
  const int tid = threadIdx.x, lane = tid & 63, w = __builtin_amdgcn_readfirstlane(tid >> 6), r = lane >> 3, c = lane & 7;
  const int pidx = (g & 1) * 32 + (hp & 3) * 8 + jq;
  const int rq = (int)(plan_role(pidx >> 4) >> ((pidx & 15) * 4)) & 15, role = rq & 3, qp = rq >> 2, ord = (int)(plan_ord(pidx >> 4) >> ((pidx & 15) * 4)) & 15;
  const bool isT1 = role == 1, isT2 = role == 2, isT3 = role == 3;
  const int j1 = ord, j2 = g * 6 + ord, j3 = (g >> 1) * 5 + (g & 1) * 3 + ord;
  const __amdgpu_buffer_rsrc_t rs = __builtin_amdgcn_make_buffer_rsrc((void*)ws, 0, WS_BYTES, 0x00020000);
  const int slot = h * 8 + b * 4 + s;
  const int G = lane >> 4, m16 = lane & 15, hb = lane >> 5, l32 = lane & 31;
  const int troff = (8 * G + (m16 >> 2)), trp = (m16 & 3) * 8;
  const int ks2 = w < 4 ? 3 * w : 4 + 2 * w, nks2 = w < 4 ? 3 : 2;
  LAS unsigned char* ob = L + L_OB + w * 384;
  LAS float* red = (LAS float*)(L + L_RED);
  volatile LAS unsigned* ctl = (volatile LAS unsigned*)(L + L_MISC);
  auto wait_ctl = [&](int k, unsigned want) { for (unsigned spin = 0; spin < (SPIN_CAP << 2) && ctl[k] != want; ++spin) __builtin_amdgcn_s_sleep(1); WAVE_SYNC(); };
  auto arrive = [&](int k) -> unsigned { LGKM0(); unsigned t = 0; if (lane == 0) t = __hip_atomic_fetch_add((LAS unsigned*)&ctl[k], 1u, __ATOMIC_RELAXED, __HIP_MEMORY_SCOPE_WORKGROUP);
                                         t = __builtin_amdgcn_readfirstlane(t); WAVE_SYNC(); return t; };
  u64 epoch64 = 0;
  if (w < 4) {
    u32x2 ep;
    if (w == 0 && lane < 16) ctl[lane] = 0u;
    if (qp) {
      u32x2 xa[5]; unsigned xk[5];
      { const int v1 = hb * (D * 2) + l32 * 8, v2 = hb * (D * 2) + (160 * w + l32) * 2;
        asm volatile("global_load_dwordx2 %0, %10, %12\n\tglobal_load_dwordx2 %1, %10, %12 offset:256\n\tglobal_load_dwordx2 %2, %10, %12 offset:512\n\t"
                     "global_load_dwordx2 %3, %10, %12 offset:768\n\tglobal_load_dwordx2 %4, %10, %12 offset:1024\n\t"
                     "global_load_ushort %5, %11, %12\n\tglobal_load_ushort %6, %11, %12 offset:64\n\tglobal_load_ushort %7, %11, %12 offset:128\n\t"
                     "global_load_ushort %8, %11, %12 offset:192\n\tglobal_load_ushort %9, %11, %12 offset:256"
                     : "=&v"(xa[0]), "=&v"(xa[1]), "=&v"(xa[2]), "=&v"(xa[3]), "=&v"(xa[4]), "=&v"(xk[0]), "=&v"(xk[1]), "=&v"(xk[2]), "=&v"(xk[3]), "=&v"(xk[4])
                     : "v"(v1), "v"(v2), "s"(x) : "memory"); }
      { const char* sq = (const char*)wq + (size_t)(160 * w + (lane >> 3)) * (HD * 2) + h * (DH * 2) + (qp - 1) * 128 + (lane & 7) * 16;
#pragma unroll
        for (int n = 0; n < 20; ++n) glds16(sq + (size_t)n * (8 * HD * 2), lbase + L_WA + w * 20480 + n * 1024); }
      { const int vz = 0; asm volatile("global_load_dwordx2 %0, %1, %2 sc1" : "=&v"(ep) : "v"(vz), "s"(ws) : "memory"); }
      LGKM0();
      __builtin_amdgcn_s_barrier();
      VMWAIT(21);
      asm volatile("" : "+v"(xa[0]), "+v"(xa[1]), "+v"(xa[2]), "+v"(xa[3]), "+v"(xa[4]), "+v"(xk[0]), "+v"(xk[1]), "+v"(xk[2]), "+v"(xk[3]), "+v"(xk[4]) :: "memory");
      LAS unsigned char* obq = L + L_OB + w * 640;
      { f32x2 hv[10]; float hk[5];
#pragma unroll
        for (int n = 0; n < 5; ++n) { hv[2 * n] = f32x2{bflo(xa[n][0]), bfhi(xa[n][0])}; hv[2 * n + 1] = f32x2{bflo(xa[n][1]), bfhi(xa[n][1])}; hk[n] = bflo(xk[n]); }
        f32x2 s2 = hv[0];
#pragma unroll
        for (int n = 1; n < 10; ++n) s2 += hv[n];
        const float mean = half_sum(s2[0] + s2[1]) * (1.0f / D);
        s2 = f32x2{0.f, 0.f};
#pragma unroll
        for (int n = 0; n < 10; ++n) { const f32x2 dd = hv[n] - mean; s2 = __builtin_elementwise_fma(dd, dd, s2); }
        ln_store<5>(mean, half_sum(s2[0] + s2[1]) * (1.0f / D), hk, obq, lane); }
      DRAIN();
      asm volatile("" : "+v"(ep) :: "memory");
      epoch64 = (((u64)ep[1] << 32) | ep[0]) + 1ull;
      { bf16x8 bw[5][4];
#pragma unroll
        for (int ks = 0; ks < 5; ++ks)
#pragma unroll
          for (int ct = 0; ct < 4; ++ct) bw[ks][ct] = trfrag(L + L_WA + (160 * w + 32 * ks + troff) * 128 + ct * 32 + trp, 128);
        gemv2<5, 4>(obq, bw, 5, red + w, lane); }
      if (arrive(4) == 3u) {
#pragma unroll
        for (int n = 0; n < 2; ++n) {
          const f32x4 t = *(const LAS f32x4*)(red + (n * 64 + lane) * 8);
          __hip_atomic_store((GAS u64*)(ws + WS_QG) + ((G & 1) * H + h) * DH + (qp - 1) * 64 + n * 32 + hb * 16 + m16, (epoch64 << 16) | (u64)f2bf((t[0] + t[1]) + (t[2] + t[3])), __ATOMIC_RELAXED, __HIP_MEMORY_SCOPE_AGENT);
        }
        ctl[6] = 1u;
      }
    } else {
      epoch64 = __hip_atomic_load((GAS u64*)(ws + WS_EPOCH), __ATOMIC_RELAXED, __HIP_MEMORY_SCOPE_AGENT);
      LGKM0();
      __builtin_amdgcn_s_barrier();
      epoch64 += 1ull;
    }
    if (w == 1) {
      u64 qg[3];
      for (unsigned spin = 0; spin < SPIN_CAP; ++spin) {
        u64 bad = 0;
#pragma unroll
        for (int n = 0; n < 3; ++n) { qg[n] = __hip_atomic_load((GAS u64*)(ws + WS_QG) + (b * H + h) * DH + n * 64 + lane, __ATOMIC_RELAXED, __HIP_MEMORY_SCOPE_AGENT); }
#pragma unroll
        for (int n = 0; n < 3; ++n) bad |= (qg[n] ^ (epoch64 << 16)) >> 16;
        if (__all(bad == 0)) break;
        __builtin_amdgcn_s_sleep(8);
      }
#pragma unroll
      for (int n = 0; n < 3; ++n) ((LAS unsigned short*)(L + L_QBF))[n * 64 + lane] = (unsigned short)qg[n];
      LGKM0();
      ctl[0] = 1u;
    }
    if (w == 0) {
      const u32x4 sv = {SENT, SENT, SENT, SENT};
      if (lane < 48) __builtin_amdgcn_raw_buffer_store_b128(sv, rs, WS_PA + slot * PA_STRIDE + lane * 16, 0, AUX_SC1);
      if (isT1) {
        __builtin_amdgcn_raw_buffer_store_b128(sv, rs, WS_PB + hb * PB_REP + ((g * 2 + (G & 1)) * D + 64 * j1) * 4 + m16 * 16, 0, AUX_SC1);
      } else if (isT2 && lane < 8 * NREP) __builtin_amdgcn_raw_buffer_store_b128(sv, rs, WS_ACT + (lane >> 3) * ACT_REP + j2 * 128 + (lane & 7) * 16, 0, AUX_SC1);
      DRAIN();
      ctl[8] = 1u;
      if ((isT1 || isT2) && lane < 16) __hip_atomic_store((GAS u64*)(ws + WS_FLAGS + (isT1 ? g * 10 + j1 : 80 + j2) * 128) + lane, epoch64, __ATOMIC_RELAXED, __HIP_MEMORY_SCOPE_AGENT);
      for (unsigned spin = 0; spin < SPIN_CAP && ctl[1] != NS; ++spin) __builtin_amdgcn_s_sleep(8);
      WAVE_SYNC();
      f32x2 ml[NS];
#pragma unroll
      for (int n = 0; n < NS; ++n) ml[n] = *(const LAS f32x2*)(L + L_MRG + n * MRG_STRIDE);
      const float M = fmaxf(fmaxf(ml[0][0], ml[1][0]), fmaxf(ml[2][0], ml[3][0]));
      float Ls = 0.f;
#pragma unroll
      for (int n = 0; n < NS; ++n) Ls = __builtin_fmaf(__builtin_amdgcn_exp2f((ml[n][0] - fin(M)) * LOG2E), ml[n][1], Ls);
      if (lane < 16) __hip_atomic_store((GAS u64*)(ws + WS_PA + slot * PA_STRIDE + 768) + lane, (epoch64 << 32) | fbits((lane & 1) ? Ls : M), __ATOMIC_RELAXED, __HIP_MEMORY_SCOPE_AGENT);
      if (isT2 || isT3) {
        for (unsigned spin = 0; spin < (SPIN_CAP >> 3); ++spin) {
          const u64 f0 = __hip_atomic_load((GAS u64*)(ws + WS_FLAGS + lane * 128), __ATOMIC_RELAXED, __HIP_MEMORY_SCOPE_AGENT);
          const u64 f1 = __hip_atomic_load((GAS u64*)(ws + WS_FLAGS + (64 + (isT3 ? lane : m16)) * 128), __ATOMIC_RELAXED, __HIP_MEMORY_SCOPE_AGENT);
          if (__all(f0 == epoch64 && f1 == epoch64)) break;
          __builtin_amdgcn_s_sleep(100);
        }
        ctl[2] = 1u;
      }
    }
  } else {
    const int sw = w - 4;
    const unsigned quad = (unsigned)(plan_tiles(pidx >> 3) >> (b * 32));
    const int nt = (quad >> (8 * s)) & 255, t0 = (s > 0 ? quad & 255 : 0) + (s > 1 ? (quad >> 8) & 255 : 0) + (s > 2 ? (quad >> 16) & 255 : 0);
    const int m = ((nt - sw + NS - 1) / NS) | 1;
    const size_t koff = ((size_t)(b * H + h) * SK + (size_t)t0 * TK) * (DH * 2);
    const __amdgpu_buffer_rsrc_t rk = __builtin_amdgcn_make_buffer_rsrc((void*)((const char*)Kc + koff), 0, nt * TBY, 0x00020000);
    const __amdgpu_buffer_rsrc_t rv = __builtin_amdgcn_make_buffer_rsrc((void*)((const char*)Vc + koff), 0, nt * TBY, 0x00020000);
    const int loff = r * 384 + c * 16;
    const int rot = (((70 * bi - 3 * t0) & 255) * 171) >> 9;
    auto tile = [&](int p) { const int u = p * NS + sw, t = u + rot; return u >= nt ? nt : t >= nt ? t - nt : t; };
#define LDKV(rs_, so_, u) __builtin_amdgcn_raw_buffer_load_b128(rs_, loff + OFF(u), so_, AUX_NT)
    __builtin_amdgcn_s_barrier();
    auto fetch_w = [&]() { if (isT1 || isT2 || isT3) {
      const char* p; size_t st; int np = 24;
      if (isT1) { p = (const char*)wo + (size_t)(ghead(sw) * DH + (lane >> 3)) * (D * 2) + j1 * 128 + (lane & 7) * 16; st = 8 * D * 2; }
      else if (isT2) { p = (const char*)((lane & 4) ? wu : wg) + (size_t)(160 * sw + (lane >> 3)) * (F * 2) + j2 * 64 + (lane & 3) * 16; st = 8 * F * 2; np = 20; }
      else { p = (const char*)wd + (size_t)(384 * sw + (lane >> 2)) * (D * 2) + j3 * 64 + (lane & 3) * 16; st = 16 * D * 2; }
#pragma unroll 1
      for (int n = 0; n < np; ++n) glds16(p + (size_t)n * st, lbase + L_WA + (sw * np + n) * 1024);
    } };
    u32x4 a[W];
    u32x4 ex[12];
    { const int p0 = tile(0) * TBY, p1 = tile(1) * TBY, p2 = tile(m - 1) * TBY;
#pragma unroll
      for (int u = 0; u < 12; ++u) {
        if (u == PRE) { if (qp) wait_ctl(6, 1u); else __builtin_amdgcn_s_sleep(DELAY0); fetch_w(); }
        ex[u] = LDKV(rk, p2, u); __builtin_amdgcn_sched_barrier(0); }
#pragma unroll
      for (int u = 0; u < W; ++u) { a[u] = LDKV(rk, u < 12 ? p0 : p1, u % 12); __builtin_amdgcn_sched_barrier(0); } }
    wait_ctl(0, 1u);
    u32x4 qv[3];
#pragma unroll
    for (int j = 0; j < 3; ++j) qv[j] = *(const LAS u32x4*)(L + L_QBF + (j * 8 + c) * 16);
    const float scale = 0.07216878364870322f;
    float mx = -INFINITY;
    LAS float* scw = (LAS float*)(L + L_SC) + sw * SCW;
    f32x2 acc[12];
#define K_TILE(REG, HALF, p, REFILL) do { const __amdgpu_buffer_rsrc_t rn_ = (p) + 3 < m ? rk : rv; const int nx_ = tile((p) + 3 < m ? (p) + 2 : (p) + 3 - m) * TBY; float d_[4]; \
      _Pragma("unroll") for (int gg = 0; gg < 4; ++gg) { float t_ = 0.f; \
        _Pragma("unroll") for (int j = 0; j < 3; ++j) { const int u = (HALF) * 12 + gg * 3 + j; \
          _Pragma("unroll") for (int e = 0; e < 4; ++e) t_ = dot2(REG[u][e], qv[j][e], t_); \
          if (REFILL) { REG[u] = LDKV(rn_, nx_, gg * 3 + j); __builtin_amdgcn_sched_barrier(0); } } \
        d_[gg] = t_; } \
      _Pragma("unroll") for (int n = 0; n < 4; ++n) d_[n] += dpp<HMIRROR>(d_[n]); \
      _Pragma("unroll") for (int n = 0; n < 2; ++n) { const float t1 = d_[n] + dpp<XOR2>(d_[n]), t2 = d_[n + 2] + dpp<XOR2>(d_[n + 2]); d_[n] = (c & 2) ? t2 : t1; } \
      const float t1_ = d_[0] + dpp<XOR1>(d_[0]), t2_ = d_[1] + dpp<XOR1>(d_[1]); \
      const float sv_ = tile(p) < nt ? rbf(rbf((c & 1) ? t2_ : t1_) * scale) : -INFINITY; \
      mx = fmaxf(mx, sv_); \
      if (c < 4) scw[(p) * TK + r * 4 + c] = sv_; } while (0)
#define V_TILE(HALF, p, REFILL) do { const int nx_ = tile((REFILL) ? (p) + 2 : 0) * TBY; \
      const f32x4 pv_ = *(const LAS f32x4*)&scw[(p) * TK + r * 4]; \
      _Pragma("unroll") for (int v = 0; v < 12; ++v) { const int u = (HALF) * 12 + v; fma_row(acc, v % 3, a[u], pv_[v / 3]); \
        if (REFILL) { asm volatile("" : "+v"(acc[(v % 3) * 4]), "+v"(acc[(v % 3) * 4 + 1]), "+v"(acc[(v % 3) * 4 + 2]), "+v"(acc[(v % 3) * 4 + 3])); __builtin_amdgcn_sched_barrier(0); a[u] = LDKV(rv, nx_, v); __builtin_amdgcn_sched_barrier(0); } } } while (0)
    K_TILE(ex, 0, m - 1, false);
    __builtin_amdgcn_sched_barrier(0);
#pragma unroll 1
    for (int p = 0; p + 1 < m; p += 2) { K_TILE(a, 0, p, true); K_TILE(a, 1, p + 1, true); }
    mx = wave_max(mx);
    WAVE_SYNC();
    float ls = 0.f;
    const float mxs = fin(mx);
#pragma unroll
    for (int n = 0; n < SCW / 64; ++n) { float p = __builtin_amdgcn_exp2f((scw[n * 64 + lane] - mxs) * LOG2E); p = n * 64 + lane < m * TK ? p : 0.f; scw[n * 64 + lane] = p; ls += p; }
    ls = wave_sum(ls);
    { LAS float* mr = (LAS float*)(L + L_MRG + sw * MRG_STRIDE); if (lane == 0) { mr[0] = mx; mr[1] = ls; } }
    arrive(1);
#pragma unroll
    for (int n = 0; n < 12; ++n) acc[n] = f32x2{0.f, 0.f};
#pragma unroll 1
    for (int p = 0; p + 3 < m; p += 2) { V_TILE(0, p, true); V_TILE(1, p + 1, true); }
    V_TILE(0, m - 3, true);
    wait_ctl(1, NS);
    wait_ctl(8, 1u);
    float wsc;
    { const LAS float* mr = (const LAS float*)(L + L_MRG);
      wsc = __builtin_amdgcn_exp2f((mx - fin(fmaxf(fmaxf(mr[0], mr[MRG_STRIDE / 4]), fmaxf(mr[2 * MRG_STRIDE / 4], mr[3 * MRG_STRIDE / 4])))) * LOG2E); }
    int dco[3], pao = WS_PA + slot * PA_STRIDE + lane * 16;
#pragma unroll
    for (int n = 0; n < 3; ++n) { dco[n] = L_MRG + sw * MRG_STRIDE + 16 + reduce_col(lane, n) * 4; PIN(dco[n]); }
    PIN(wsc); PIN(pao);
    V_TILE(1, m - 2, false); V_TILE(0, m - 1, false);
    reduce_rows(acc, wsc, L, dco, lane);
    const unsigned arr = arrive(3);
    if (arr == NS - 1 && lane < 48) {
      f32x4 ov[NS];
#pragma unroll
      for (int n = 0; n < NS; ++n) ov[n] = *(const LAS f32x4*)(L + L_MRG + n * MRG_STRIDE + 16 + 16 * lane);
      __builtin_amdgcn_raw_buffer_store_b128(desent4((ov[0] + ov[1]) + (ov[2] + ov[3])), rs, pao, 0, AUX_SC1);
    }
  }
  const int rep = (bid >> 2) & 1;
  auto wait_slow = [&](int k, unsigned want) { for (unsigned spin = 0; spin < SPIN_CAP && ctl[k] != want; ++spin) __builtin_amdgcn_s_sleep(8); WAVE_SYNC(); };
  if (!(isT1 || isT2 || isT3)) return;
  wait_slow(1, NS);
  if (isT1) {
    if (w >= 4) return;
    bf16x8 bw[6][4];
#pragma unroll
    for (int ks = 0; ks < 6; ++ks)
#pragma unroll
      for (int ct = 0; ct < 4; ++ct) bw[ks][ct] = trfrag(L + L_WA + (192 * w + 32 * ks + troff) * 128 + ct * 32 + trp, 128);
    const int so = WS_PA + ghead(w) * 8 * PA_STRIDE, lc = lane < 48 ? lane : 47;
    float wt[8], Ls[2] = {0.f, 0.f}, rl[2];
    { u64 ml[16];
      for (unsigned spin = 0; spin < (SPIN_CAP >> 3); ++spin) {
        unsigned bad = 0u;
#pragma unroll
        for (int n = 0; n < 16; ++n) ml[n] = __hip_atomic_load((GAS u64*)(ws + so + (n >> 1) * PA_STRIDE + 768) + (n & 1), __ATOMIC_RELAXED, __HIP_MEMORY_SCOPE_AGENT);
#pragma unroll
        for (int n = 0; n < 16; ++n) bad |= (unsigned)(ml[n] >> 32) ^ (unsigned)epoch64;
        if (__all(bad == 0u)) break;
        __builtin_amdgcn_s_sleep(64);
      }
#pragma unroll
      for (int bb = 0; bb < 2; ++bb) {
        const float M = fmaxf(fmaxf(bitsf((unsigned)ml[8 * bb]), bitsf((unsigned)ml[8 * bb + 2])), fmaxf(bitsf((unsigned)ml[8 * bb + 4]), bitsf((unsigned)ml[8 * bb + 6])));
#pragma unroll
        for (int n = 4 * bb; n < 4 * bb + 4; ++n) { wt[n] = __builtin_amdgcn_exp2f((bitsf((unsigned)ml[2 * n]) - M) * LOG2E); Ls[bb] = __builtin_fmaf(wt[n], bitsf((unsigned)ml[2 * n + 1]), Ls[bb]); }
        rl[bb] = __builtin_amdgcn_rcpf(Ls[bb]);
#pragma unroll
        for (int n = 4 * bb; n < 4 * bb + 4; ++n) { wt[n] = fdiv(wt[n], Ls[bb], rl[bb]); PIN(wt[n]); } } }
    int pbo = WS_PB + ((g * 2 + (G & 1)) * D + 64 * j1 + hb * 16 + m16) * 4;
    PIN(pbo);
    wait_ctl(3, NS);
    u32x4 o4[8];
    poll<8>(o4, [&](int n) { return __builtin_amdgcn_raw_buffer_load_b128(rs, so + n * PA_STRIDE + lc * 16, 0, AUX_SC1); });
    LAS unsigned char* ob1 = L + L_OB + w * 768;
#pragma unroll
    for (int bb = 0; bb < 2; ++bb) {
      f32x4 o = wt[4 * bb] * __builtin_bit_cast(f32x4, o4[4 * bb]);
#pragma unroll
      for (int n = 4 * bb + 1; n < 4 * bb + 4; ++n) o += wt[n] * __builtin_bit_cast(f32x4, o4[n]);
      const u32x2 obv = {pk2(o[0], o[1]), pk2(o[2], o[3])};
      if (lane < 48) *(LAS u32x2*)(ob1 + bb * 384 + lane * 8) = obv; }
    WAVE_SYNC();
    gemv2<6, 4>(ob1, bw, 6, red + w, lane);
    if (arrive(7) == 3u) {
#pragma unroll
      for (int half = 0; half < 2; ++half) {
        const f32x4 t = *(const LAS f32x4*)(red + (half * 64 + lane) * 8);
        const unsigned tv = desent(fbits((t[0] + t[1]) + (t[2] + t[3])));
#pragma unroll
        for (int n = 0; n < NREP; ++n)
          __builtin_amdgcn_raw_buffer_store_b32(tv, rs, pbo + half * 128, n * PB_REP, AUX_SC1);
      }
    }
  } else if (isT3) {
    bf16x8 bd[6][2];
#pragma unroll
    for (int ks = 0; ks < 6; ++ks)
#pragma unroll
      for (int ct = 0; ct < 2; ++ct) bd[ks][ct] = trfrag(L + L_WA + (192 * w + 32 * ks + troff) * 64 + ct * 32 + trp, 64);
    wait_slow(2, 1u);
    wait_ctl(3, NS);
    if (w == 2) {
      const int b3 = (lane >> 3) & 1, f3 = lane & 7;
      const u32x2 xr = *(const GAS u32x2*)(x + b3 * D + 32 * j3 + 4 * f3);
      u32x4 pb[8];
      for (unsigned spin = 0; spin < SPIN_CAP; ++spin) {
        unsigned mxv = 0u;
#pragma unroll
        for (int n = 0; n < 8; ++n) pb[n] = __builtin_amdgcn_raw_buffer_load_b128(rs, WS_PB + rep * PB_REP + ((n * 2 + b3) * D + 32 * j3 + 4 * f3) * 4, 0, AUX_SC1);
#pragma unroll
        for (int n = 0; n < 8; ++n) mxv = umax4(mxv, pb[n]);
        if (!__any(mxv == SENT)) break;
        __builtin_amdgcn_s_sleep(2);
      }
      ctl[5] = 1u;
      const f32x4 t = tree8(pb);
      if (lane < 16) *(LAS f32x4*)(L + L_H3 + lane * 16) = f32x4{rbf(bflo(xr[0]) + rbf(t[0])), rbf(bfhi(xr[0]) + rbf(t[1])), rbf(bflo(xr[1]) + rbf(t[2])), rbf(bfhi(xr[1]) + rbf(t[3]))};
    } else wait_ctl(5, 1u);
    u32x4 av[6];
    poll<6>(av, [&](int ks) { return __builtin_amdgcn_raw_buffer_load_b128(rs, WS_ACT + rep * ACT_REP + (6 * w + ks) * 128 + (m16 & 1) * 64 + G * 16, 0, AUX_SC1); });
    f32x4 cc[4];
#pragma unroll
    for (int n = 0; n < 4; ++n) cc[n] = f32x4{0.f, 0.f, 0.f, 0.f};
#pragma unroll
    for (int ks = 0; ks < 3; ++ks)
#pragma unroll
      for (int ct = 0; ct < 2; ++ct) { cc[ct] = mfma(av[ks], bd[ks][ct], cc[ct]); cc[2 + ct] = mfma(av[ks + 3], bd[ks + 3][ct], cc[2 + ct]); }
    if (lane < 16) {
#pragma unroll
      for (int ct = 0; ct < 2; ++ct) { red[(ct * 32 + lane) * 8 + w] = cc[ct][0] + cc[2 + ct][0]; red[(ct * 32 + 16 + lane) * 8 + w] = cc[ct][1] + cc[2 + ct][1]; } }
    __syncthreads();
    if (w == 0) {
      const float t = sum8(*(const LAS f32x4*)(red + lane * 8), *(const LAS f32x4*)(red + lane * 8 + 4));
      const float ov = ((const LAS float*)(L + L_H3))[(G & 1) * 32 + hb * 16 + m16] + rbf(t);
      const unsigned pk = pk2(ov, dpp<XOR1>(ov));
      if (!(lane & 1)) *(GAS unsigned*)(out + (G & 1) * D + 32 * j3 + hb * 16 + m16) = pk;
      if (j3 == 0 && lane == 0) __hip_atomic_store((GAS u64*)(ws + WS_EPOCH), epoch64, __ATOMIC_RELAXED, __HIP_MEMORY_SCOPE_AGENT);
    }
  } else if (isT2) {
    const int bb = w >> 2, idx = (w & 3) * 40 + (lane < 40 ? lane : 39);
    const u32x2 xr = *(const GAS u32x2*)(x + bb * D + 4 * idx);
    bf16x8 bw[3][4];
#pragma unroll
    for (int ks = 0; ks < 3; ++ks)
#pragma unroll
      for (int ct = 0; ct < 4; ++ct) bw[ks][ct] = trfrag(L + L_WA + (32 * (ks2 + (ks < nks2 ? ks : 0)) + troff) * 128 + ct * 32 + trp, 128);
    f32x4 xf = {bflo(xr[0]), bfhi(xr[0]), bflo(xr[1]), bfhi(xr[1])};
    int ato = WS_ACT + w * ACT_REP + j2 * 128 + (G & 1) * 64 + hb * 32 + m16 * 2;
    PIN(xf); PIN(ato);
    wait_slow(2, 1u);
    wait_ctl(3, NS);
    u32x4 pb[8];
    unsigned pend = 0xFFu;
    auto ldpb = [&](int n) { return __builtin_amdgcn_raw_buffer_load_b128(rs, WS_PB + rep * PB_REP + (bb * D + 4 * idx) * 4, n * (2 * D * 4), AUX_SC1); };
    for (unsigned spin = 0; spin < SPIN_CAP; ++spin) {
#pragma unroll
      for (int n = 0; n < 8; ++n) if (pend & (1u << n)) pb[n] = ldpb(n);
#pragma unroll
      for (int n = 0; n < 8; ++n) if ((pend & (1u << n)) && !__any(umax4(0u, pb[n]) == SENT)) pend &= ~(1u << n);
      if (pend == 0u) break;
      __builtin_amdgcn_s_sleep(1);
    }
    { const f32x4 t = tree8(pb);
      const f32x4 hq = {rbf(xf[0] + rbf(t[0])), rbf(xf[1] + rbf(t[1])), rbf(xf[2] + rbf(t[2])), rbf(xf[3] + rbf(t[3]))};
      if (lane < 40) *(LAS f32x4*)(L + L_H + (bb * D + 4 * idx) * 4) = hq;
      float s1 = (hq[0] + hq[1]) + (hq[2] + hq[3]), s2 = __builtin_fmaf(hq[0], hq[0], hq[1] * hq[1]) + __builtin_fmaf(hq[2], hq[2], hq[3] * hq[3]);
      s1 = lane < 40 ? s1 : 0.f; s2 = lane < 40 ? s2 : 0.f;
      s1 = row_sum(s1); s2 = row_sum(s2);
      const float t12 = swap16_sum(s1, s2);
      const float tt = swap32_sum(t12, t12);
      if ((lane & 47) == 0) ((LAS float*)(L + L_ST))[((lane >> 4) & 1) * 8 + w] = tt; }
    __syncthreads();
    float hk[3];
#pragma unroll
    for (int ks = 0; ks < 3; ++ks) hk[ks] = ((const LAS float*)(L + L_H))[hb * D + 32 * (ks2 + (ks < nks2 ? ks : 0)) + l32];
    { const f32x4 p1 = *(const LAS f32x4*)(L + L_ST + hb * 16), p2 = *(const LAS f32x4*)(L + L_ST + 32 + hb * 16);
      const float mean = ((p1[0] + p1[1]) + (p1[2] + p1[3])) * (1.0f / D);
      const float var = fmaxf(__builtin_fmaf(-mean, mean, ((p2[0] + p2[1]) + (p2[2] + p2[3])) * (1.0f / D)), 0.f);
      ln_store<3>(mean, var, hk, ob, lane); }
    gemv2<3, 4>(ob, bw, nks2, red + w, lane);
    __syncthreads();
    if (w < NREP) {
      const float gs = rbf(sum8(*(const LAS f32x4*)(red + lane * 8), *(const LAS f32x4*)(red + lane * 8 + 4)));
      const float us = rbf(sum8(*(const LAS f32x4*)(red + (64 + lane) * 8), *(const LAS f32x4*)(red + (64 + lane) * 8 + 4)));
      const float inner = 0.7978845608028654f * (gs + 0.044715f * gs * gs * gs);
      const float dn = 1.0f + __builtin_amdgcn_exp2f(-2.0f * LOG2E * inner);
      const float ge = rbf(gs * __builtin_amdgcn_rcpf(dn));
      const float av = ge * us;
      const unsigned pk = desent(pk2(av, dpp<XOR1>(av)));
      if (!(lane & 1)) __builtin_amdgcn_raw_buffer_store_b32(pk, rs, ato, 0, AUX_SC1);
    }
  }
  asm volatile(".Lfb_code_end:");
}

extern "C" void kernel_launch(void* const* d_in, const int* in_sizes, int n_in, void* d_out, int out_size, void* d_ws, size_t ws_size, hipStream_t stream) {
  (void)in_sizes; (void)n_in; (void)out_size; (void)ws_size;
  (void)hipFuncSetAttribute((const void*)fused_block, hipFuncAttributeMaxDynamicSharedMemorySize, L_BYTES);
  hipLaunchKernelGGL(fused_block, dim3(NWG), dim3(NTHR), L_BYTES, stream, (const bf16_t*)d_in[0], (const bf16_t*)d_in[1], (const bf16_t*)d_in[2],
                     (const bf16_t*)d_in[3], (const bf16_t*)d_in[4], (const bf16_t*)d_in[6], (const bf16_t*)d_in[7], (const bf16_t*)d_in[8],
                     (bf16_t*)d_out, (unsigned char*)d_ws);
}
```
